```python
import torch

def _softmax(x, axis=-1):
    return torch.softmax(x, dim=axis)
def _gelu(x):
    return torch.nn.functional.gelu(x, approximate="tanh")
def _silu(x):
    return torch.nn.functional.silu(x)
def _sigmoid(x):
    return torch.sigmoid(x)
def _layernorm(x, axis=-1, eps=1e-5):
    m = x.mean(dim=axis, keepdim=True)
    v = x.var(dim=axis, keepdim=True, unbiased=False)
    return (x - m) / torch.sqrt(v + eps)
def _scatter_add(tgt, idx, src, axis=0):
    return tgt.clone().index_add_(axis, idx, src)
def _segment_sum(src, seg, n):
    out = torch.zeros((n,) + src.shape[1:], dtype=src.dtype)
    return out.index_add_(0, seg, src)
def _pad(x, pw):
    flat = tuple(v for pair in reversed(pw) for v in pair)
    return torch.nn.functional.pad(x, flat)

kernel_name = 'standalone__grn__sampling'

def setup_inputs():

    _w = (lambda a: (lambda t: t.to(torch.bfloat16) if t.is_floating_point() else t)(torch.as_tensor(a)))
    B, S, D = 1024, 32, 512
    return {
        "x": _w((lambda *s: torch.randn(*s, dtype=torch.bfloat16) * 0.05)(B, S, D)),
        "g": _w((lambda *s: torch.randn(*s, dtype=torch.bfloat16) * 0.4)(D) * 0.1),
        "b": _w((lambda *s: torch.randn(*s, dtype=torch.bfloat16) * 0.1)(D) * 0.1),
    }

def reference(x, g, b):
    gx = torch.sqrt((x * x).sum(axis=-2, keepdims=True))
    nx = gx / (gx.mean(axis=-1, keepdims=True) + 1e-6)
    return g * (x * nx) + b + x
```

```cpp
#include <hip/hip_runtime.h>
#include <hip/hip_bf16.h>

#define WPB @WPB@
#define BPB @BPB@
#define LD_AUX @LDAUX@
#define ST_AUX @STAUX@

typedef unsigned int u32;
typedef u32 u32x4 __attribute__((ext_vector_type(4)));
typedef float f32x2 __attribute__((ext_vector_type(2)));
typedef __bf16 bf16x2 __attribute__((ext_vector_type(2)));

constexpr int S = 32, D = 512;
constexpr int RPW = S / WPB;
constexpr int THREADS = 64 * WPB * BPB;

__device__ __forceinline__ float lo_f(u32 v) { return __builtin_bit_cast(float, v << 16); }
__device__ __forceinline__ float hi_f(u32 v) { return __builtin_bit_cast(float, v & 0xffff0000u); }
__device__ __forceinline__ u32 pack2(float a, float b) {
    f32x2 f = {a, b};
    bf16x2 h = __builtin_convertvector(f, bf16x2);
    return __builtin_bit_cast(u32, h);
}

__device__ __forceinline__ float wave_sum(float v) {
    v += __builtin_bit_cast(float, __builtin_amdgcn_update_dpp(0, __builtin_bit_cast(int, v), 0xB1, 0xf, 0xf, true));
    v += __builtin_bit_cast(float, __builtin_amdgcn_update_dpp(0, __builtin_bit_cast(int, v), 0x4E, 0xf, 0xf, true));
    v += __builtin_bit_cast(float, __builtin_amdgcn_update_dpp(0, __builtin_bit_cast(int, v), 0x141, 0xf, 0xf, true));
    v += __builtin_bit_cast(float, __builtin_amdgcn_update_dpp(0, __builtin_bit_cast(int, v), 0x140, 0xf, 0xf, true));
    float a = __builtin_bit_cast(float, __builtin_amdgcn_readlane(__builtin_bit_cast(int, v), 0));
    float b = __builtin_bit_cast(float, __builtin_amdgcn_readlane(__builtin_bit_cast(int, v), 16));
    float c = __builtin_bit_cast(float, __builtin_amdgcn_readlane(__builtin_bit_cast(int, v), 32));
    float d = __builtin_bit_cast(float, __builtin_amdgcn_readlane(__builtin_bit_cast(int, v), 48));
    return (a + b) + (c + d);
}

__global__ __launch_bounds__(THREADS) void grn_kernel(const u32x4* __restrict__ x, const u32x4* __restrict__ g,
                                                      const u32x4* __restrict__ bia, u32x4* __restrict__ out) {
    const int tid = threadIdx.x;
    const int lane = tid & 63;
    const int wv = __builtin_amdgcn_readfirstlane(tid >> 6);
    const int bl = wv / WPB;
    const int wr = wv % WPB;
    const int batch = blockIdx.x * BPB + bl;
    const char* xb = (const char*)x + (size_t)batch * (S * D * 2) + (size_t)wr * (RPW * D * 2);
    char* ob = (char*)out + (size_t)batch * (S * D * 2) + (size_t)wr * (RPW * D * 2);
    __amdgpu_buffer_rsrc_t rx = __builtin_amdgcn_make_buffer_rsrc((void*)xb, 0, RPW * D * 2, 0x00020000);
    __amdgpu_buffer_rsrc_t ro = __builtin_amdgcn_make_buffer_rsrc((void*)ob, 0, RPW * D * 2, 0x00020000);
    const int voff = lane * 16;

    u32x4 v[RPW];
#pragma unroll
    for (int r = 0; r < RPW; ++r)
        v[r] = __builtin_bit_cast(u32x4, __builtin_amdgcn_raw_buffer_load_b128(rx, voff + (r & 3) * 1024, (r >> 2) * 4096, LD_AUX));
    const u32x4 gv = g[lane];
    const u32x4 bv = bia[lane];

    float ss[8];
#pragma unroll
    for (int j = 0; j < 8; ++j) ss[j] = 0.f;
#pragma unroll
    for (int r = 0; r < RPW; ++r) {
#pragma unroll
        for (int j = 0; j < 4; ++j) {
            float a = lo_f(v[r][j]), c = hi_f(v[r][j]);
            ss[2 * j] = __builtin_fmaf(a, a, ss[2 * j]);
            ss[2 * j + 1] = __builtin_fmaf(c, c, ss[2 * j + 1]);
        }
    }
#if WPB > 1
    __shared__ float red[BPB][WPB][2][64 * 4];
    {
        float4 p0 = {ss[0], ss[1], ss[2], ss[3]}, p1 = {ss[4], ss[5], ss[6], ss[7]};
        *(float4*)&red[bl][wr][0][lane * 4] = p0;
        *(float4*)&red[bl][wr][1][lane * 4] = p1;
    }
    __syncthreads();
#pragma unroll
    for (int j = 0; j < 8; ++j) ss[j] = 0.f;
#pragma unroll
    for (int w = 0; w < WPB; ++w) {
        float4 p0 = *(float4*)&red[bl][w][0][lane * 4];
        float4 p1 = *(float4*)&red[bl][w][1][lane * 4];
        ss[0] += p0.x; ss[1] += p0.y; ss[2] += p0.z; ss[3] += p0.w;
        ss[4] += p1.x; ss[5] += p1.y; ss[6] += p1.z; ss[7] += p1.w;
    }
#endif
    float gx[8], t = 0.f;
#pragma unroll
    for (int j = 0; j < 8; ++j) { gx[j] = __builtin_sqrtf(ss[j]); t += gx[j]; }
    t = wave_sum(t);
    const float inv = 1.0f / (t * (1.0f / D) + 1e-6f);
    float sc[8], bb[8];
#pragma unroll
    for (int j = 0; j < 4; ++j) {
        sc[2 * j] = __builtin_fmaf(lo_f(gv[j]), gx[2 * j] * inv, 1.0f);
        sc[2 * j + 1] = __builtin_fmaf(hi_f(gv[j]), gx[2 * j + 1] * inv, 1.0f);
        bb[2 * j] = lo_f(bv[j]);
        bb[2 * j + 1] = hi_f(bv[j]);
    }
#pragma unroll
    for (int r = 0; r < RPW; ++r) {
        u32x4 o;
#pragma unroll
        for (int j = 0; j < 4; ++j) {
            float a = __builtin_fmaf(lo_f(v[r][j]), sc[2 * j], bb[2 * j]);
            float c = __builtin_fmaf(hi_f(v[r][j]), sc[2 * j + 1], bb[2 * j + 1]);
            o[j] = pack2(a, c);
        }
        __builtin_amdgcn_raw_buffer_store_b128(o, ro, voff + (r & 3) * 1024, (r >> 2) * 4096, ST_AUX);
    }
}

extern "C" void kernel_launch(void* const* d_in, const int* in_sizes, int n_in,
                              void* d_out, int out_size, void* d_ws, size_t ws_size,
                              hipStream_t stream) {
    const u32x4* x = (const u32x4*)d_in[0];
    const u32x4* g = (const u32x4*)d_in[1];
    const u32x4* b = (const u32x4*)d_in[2];
    u32x4* out = (u32x4*)d_out;
    const int B = in_sizes[0] / (S * D);
    grn_kernel<<<B / BPB, THREADS, 0, stream>>>(x, g, b, out);
}
```

```cpp
#include <hip/hip_runtime.h>
#include <hip/hip_bf16.h>

#define WPB @WPB@
#define BPB @BPB@
#define LD_AUX @LDAUX@
#define ST_AUX @STAUX@
#define PROBE @PROBE@
typedef unsigned long long u64;
__device__ __forceinline__ u64 rt() { u64 t; asm volatile("s_memrealtime %0\n\ts_waitcnt lgkmcnt(0)" : "=s"(t) :: "memory"); return t; }

typedef unsigned int u32;
typedef u32 u32x4 __attribute__((ext_vector_type(4)));
typedef float f32x2 __attribute__((ext_vector_type(2)));
typedef __bf16 bf16x2 __attribute__((ext_vector_type(2)));

constexpr int S = 32, D = 512;
constexpr int RPW = S / WPB;
constexpr int THREADS = 64 * WPB * BPB;

__device__ __forceinline__ float lo_f(u32 v) { return __builtin_bit_cast(float, v << 16); }
__device__ __forceinline__ float hi_f(u32 v) { return __builtin_bit_cast(float, v & 0xffff0000u); }
__device__ __forceinline__ u32 pack2(float a, float b) {
    f32x2 f = {a, b};
    bf16x2 h = __builtin_convertvector(f, bf16x2);
    return __builtin_bit_cast(u32, h);
}

__device__ __forceinline__ float wave_sum(float v) {
    v += __builtin_bit_cast(float, __builtin_amdgcn_update_dpp(0, __builtin_bit_cast(int, v), 0xB1, 0xf, 0xf, true));
    v += __builtin_bit_cast(float, __builtin_amdgcn_update_dpp(0, __builtin_bit_cast(int, v), 0x4E, 0xf, 0xf, true));
    v += __builtin_bit_cast(float, __builtin_amdgcn_update_dpp(0, __builtin_bit_cast(int, v), 0x141, 0xf, 0xf, true));
    v += __builtin_bit_cast(float, __builtin_amdgcn_update_dpp(0, __builtin_bit_cast(int, v), 0x140, 0xf, 0xf, true));
    float a = __builtin_bit_cast(float, __builtin_amdgcn_readlane(__builtin_bit_cast(int, v), 0));
    float b = __builtin_bit_cast(float, __builtin_amdgcn_readlane(__builtin_bit_cast(int, v), 16));
    float c = __builtin_bit_cast(float, __builtin_amdgcn_readlane(__builtin_bit_cast(int, v), 32));
    float d = __builtin_bit_cast(float, __builtin_amdgcn_readlane(__builtin_bit_cast(int, v), 48));
    return (a + b) + (c + d);
}

__global__ __launch_bounds__(THREADS) void grn_kernel(const u32x4* __restrict__ x, const u32x4* __restrict__ g,
                                                      const u32x4* __restrict__ bia, u32x4* __restrict__ out) {
    u64 t0 = rt(); u64 tx = t0;
    const int tid = threadIdx.x;
    const int lane = tid & 63;
    const int wv = __builtin_amdgcn_readfirstlane(tid >> 6);
    const int bl = wv / WPB;
    const int wr = wv % WPB;
    const int batch = blockIdx.x * BPB + bl;
    const char* xb = (const char*)x + (size_t)batch * (S * D * 2) + (size_t)wr * (RPW * D * 2);
    char* ob = (char*)out + (size_t)batch * (S * D * 2) + (size_t)wr * (RPW * D * 2);
    __amdgpu_buffer_rsrc_t rx = __builtin_amdgcn_make_buffer_rsrc((void*)xb, 0, RPW * D * 2, 0x00020000);
    __amdgpu_buffer_rsrc_t ro = __builtin_amdgcn_make_buffer_rsrc((void*)ob, 0, RPW * D * 2, 0x00020000);
    const int voff = lane * 16;

    u32x4 v[RPW];
#pragma unroll
    for (int r = 0; r < RPW; ++r)
        v[r] = __builtin_bit_cast(u32x4, __builtin_amdgcn_raw_buffer_load_b128(rx, voff + (r & 3) * 1024, (r >> 2) * 4096, LD_AUX));
#if PROBE == 4
    { u32 q = v[0][0]; asm volatile("" : "+v"(q)); tx = rt(); v[0][0] = q; }
#endif
    const u32x4 gv = g[lane];
    const u32x4 bv = bia[lane];

    float ss[8];
#pragma unroll
    for (int j = 0; j < 8; ++j) ss[j] = 0.f;
#pragma unroll
    for (int r = 0; r < RPW; ++r) {
#pragma unroll
        for (int j = 0; j < 4; ++j) {
            float a = lo_f(v[r][j]), c = hi_f(v[r][j]);
            ss[2 * j] = __builtin_fmaf(a, a, ss[2 * j]);
            ss[2 * j + 1] = __builtin_fmaf(c, c, ss[2 * j + 1]);
        }
    }
#if WPB > 1
    __shared__ float red[BPB][WPB][2][64 * 4];
    {
        float4 p0 = {ss[0], ss[1], ss[2], ss[3]}, p1 = {ss[4], ss[5], ss[6], ss[7]};
        *(float4*)&red[bl][wr][0][lane * 4] = p0;
        *(float4*)&red[bl][wr][1][lane * 4] = p1;
    }
    __syncthreads();
#if PROBE == 2
    tx = rt();
#endif
#pragma unroll
    for (int j = 0; j < 8; ++j) ss[j] = 0.f;
#pragma unroll
    for (int w = 0; w < WPB; ++w) {
        float4 p0 = *(float4*)&red[bl][w][0][lane * 4];
        float4 p1 = *(float4*)&red[bl][w][1][lane * 4];
        ss[0] += p0.x; ss[1] += p0.y; ss[2] += p0.z; ss[3] += p0.w;
        ss[4] += p1.x; ss[5] += p1.y; ss[6] += p1.z; ss[7] += p1.w;
    }
#endif
    float gx[8], t = 0.f;
#pragma unroll
    for (int j = 0; j < 8; ++j) { gx[j] = __builtin_sqrtf(ss[j]); t += gx[j]; }
    t = wave_sum(t);
    const float inv = 1.0f / (t * (1.0f / D) + 1e-6f);
    float sc[8], bb[8];
#pragma unroll
    for (int j = 0; j < 4; ++j) {
        sc[2 * j] = __builtin_fmaf(lo_f(gv[j]), gx[2 * j] * inv, 1.0f);
        sc[2 * j + 1] = __builtin_fmaf(hi_f(gv[j]), gx[2 * j + 1] * inv, 1.0f);
        bb[2 * j] = lo_f(bv[j]);
        bb[2 * j + 1] = hi_f(bv[j]);
    }
#pragma unroll
    for (int r = 0; r < RPW; ++r) {
        u32x4 o;
#pragma unroll
        for (int j = 0; j < 4; ++j) {
            float a = __builtin_fmaf(lo_f(v[r][j]), sc[2 * j], bb[2 * j]);
            float c = __builtin_fmaf(hi_f(v[r][j]), sc[2 * j + 1], bb[2 * j + 1]);
            o[j] = pack2(a, c);
        }
        __builtin_amdgcn_raw_buffer_store_b128(o, ro, voff + r * 1024, 0, ST_AUX);
    }
#if PROBE == 5
    tx = rt();
#endif
    asm volatile("s_waitcnt vmcnt(0)" ::: "memory");
#if PROBE == 3
    tx = rt();
#endif
    const u64 lim = t0 + 3000 + (PROBE == 4 ? 4 : 2) * (tx - t0);
    for (int i = 0; i < 100000; ++i) { if (rt() >= lim) break; __builtin_amdgcn_s_sleep(2); }
}

extern "C" void kernel_launch(void* const* d_in, const int* in_sizes, int n_in,
                              void* d_out, int out_size, void* d_ws, size_t ws_size,
                              hipStream_t stream) {
    const u32x4* x = (const u32x4*)d_in[0];
    const u32x4* g = (const u32x4*)d_in[1];
    const u32x4* b = (const u32x4*)d_in[2];
    u32x4* out = (u32x4*)d_out;
    const int B = in_sizes[0] / (S * D);
    grn_kernel<<<B / BPB, THREADS, 0, stream>>>(x, g, b, out);
}
```

```cpp
#include <hip/hip_runtime.h>

#define WPB @WPB@
#define NSEQ @NSEQ@
#define LD_AUX @LDAUX@
#define ST_AUX @STAUX@
#define PROBE @PROBE@

typedef unsigned int u32;
typedef unsigned long long u64;
typedef u32 u32x4 __attribute__((ext_vector_type(4)));
typedef u32 u32x8 __attribute__((ext_vector_type(8)));
typedef float f32x2 __attribute__((ext_vector_type(2)));
typedef float f32x4 __attribute__((ext_vector_type(4)));
typedef __bf16 bf16x2 __attribute__((ext_vector_type(2)));

constexpr int S = 32, D = 512;
constexpr int RPW = S / WPB;
constexpr int THREADS = 64 * WPB;

#if PROBE
__device__ __forceinline__ u64 rt() { u64 t; __builtin_amdgcn_sched_barrier(0); asm volatile("s_memrealtime %0\n\ts_waitcnt lgkmcnt(0)" : "=s"(t) :: "memory"); __builtin_amdgcn_sched_barrier(0); return t; }
#endif
__device__ __forceinline__ f32x2 unpack2(u32 v) {
    return f32x2{__builtin_bit_cast(float, v << 16), __builtin_bit_cast(float, v & 0xffff0000u)};
}
__device__ __forceinline__ u32 pack2(f32x2 f) { return __builtin_bit_cast(u32, __builtin_convertvector(f, bf16x2)); }
__device__ __forceinline__ f32x2 fma2(f32x2 a, f32x2 b, f32x2 c) { return __builtin_elementwise_fma(a, b, c); }

template <int CTRL>
__device__ __forceinline__ float dpp_add(float v) {
    return v + __builtin_bit_cast(float, __builtin_amdgcn_update_dpp(0, __builtin_bit_cast(int, v), CTRL, 0xf, 0xf, true));
}
__device__ __forceinline__ float wave_sum(float v) {
    v = dpp_add<0xB1>(v);
    v = dpp_add<0x4E>(v);
    v = dpp_add<0x141>(v);
    v = dpp_add<0x140>(v);
    const int iv = __builtin_bit_cast(int, v);
    float a = __builtin_bit_cast(float, __builtin_amdgcn_readlane(iv, 0));
    float b = __builtin_bit_cast(float, __builtin_amdgcn_readlane(iv, 16));
    float c = __builtin_bit_cast(float, __builtin_amdgcn_readlane(iv, 32));
    float d = __builtin_bit_cast(float, __builtin_amdgcn_readlane(iv, 48));
    return (a + b) + (c + d);
}

__global__ __launch_bounds__(THREADS) void grn_kernel(const char* __restrict__ x_, const u32x4* __restrict__ g_,
                                                      const u32x4* __restrict__ b_, char* __restrict__ out_) {
#if PROBE
    u64 t0 = rt(), tx = t0, ty = t0;
    u32x8 ka;
    asm volatile("s_load_dwordx8 %0, %1, 0x0\n\ts_waitcnt lgkmcnt(0)" : "=s"(ka) : "s"(__builtin_amdgcn_kernarg_segment_ptr()) : "memory");
    const char* x = (const char*)(((u64)ka[1] << 32) | ka[0]);
    const u32x4* g = (const u32x4*)(((u64)ka[3] << 32) | ka[2]);
    const u32x4* bia = (const u32x4*)(((u64)ka[5] << 32) | ka[4]);
    char* out = (char*)(((u64)ka[7] << 32) | ka[6]);
#if PROBE == 6
    tx = rt();
#endif
#else
    const char* x = x_; const u32x4* g = g_; const u32x4* bia = b_; char* out = out_;
#endif
    const int lane = threadIdx.x & 63;
    const int wr = WPB > 1 ? __builtin_amdgcn_readfirstlane(threadIdx.x >> 6) : 0;
    __amdgpu_buffer_rsrc_t rx = __builtin_amdgcn_make_buffer_rsrc((void*)x, 0, 0x7fffffff, 0x00020000);
    __amdgpu_buffer_rsrc_t ro = __builtin_amdgcn_make_buffer_rsrc((void*)out, 0, 0x7fffffff, 0x00020000);
    const u32 base = blockIdx.x * (S * D * 2) + wr * (RPW * D * 2) + lane * 16;
    constexpr u32 STEP = (1024 / NSEQ) * (S * D * 2);
#if WPB > 1
    __shared__ f32x4 red[NSEQ > 1 ? 2 : 1][WPB][2][64];
#endif

    u32x4 v[NSEQ > 1 ? 2 : 1][RPW];
    auto load = [&](u32x4* d, u32 off) {
#pragma unroll
        for (int r = 0; r < RPW; ++r)
            d[r] = __builtin_bit_cast(u32x4, __builtin_amdgcn_raw_buffer_load_b128(rx, off + (r & 3) * 1024, (r >> 2) * 4096, LD_AUX));
    };
    load(v[0], base);
    const u32x4 gv = g[lane];
    const u32x4 bv = bia[lane];
    __builtin_amdgcn_sched_barrier(0);

#pragma unroll
    for (int it = 0; it < NSEQ; ++it) {
        const u32x4* cur = v[it & 1];
        if (it + 1 < NSEQ) { load(v[(it + 1) & 1], base + (it + 1) * STEP); __builtin_amdgcn_sched_barrier(0); }
        f32x2 ss[4], f[RPW][4];
#pragma unroll
        for (int r = 0; r < RPW; ++r)
#pragma unroll
            for (int j = 0; j < 4; ++j) {
                f[r][j] = unpack2(cur[r][j]);
                ss[j] = r == 0 ? f[r][j] * f[r][j] : fma2(f[r][j], f[r][j], ss[j]);
            }
#if WPB > 1
        red[it & 1][wr][0][lane] = f32x4{ss[0].x, ss[0].y, ss[1].x, ss[1].y};
        red[it & 1][wr][1][lane] = f32x4{ss[2].x, ss[2].y, ss[3].x, ss[3].y};
        asm volatile("s_waitcnt lgkmcnt(0)" ::: "memory");
        __builtin_amdgcn_s_barrier();
        asm volatile("" ::: "memory");
#if PROBE == 2 || PROBE == 8
        if (it == NSEQ - 1) { if (PROBE == 2) tx = rt(); else ty = rt(); }
#endif
        {
            f32x4 p0 = red[it & 1][0][0][lane], p1 = red[it & 1][0][1][lane];
#pragma unroll
            for (int w = 1; w < WPB; ++w) { p0 += red[it & 1][w][0][lane]; p1 += red[it & 1][w][1][lane]; }
            ss[0] = f32x2{p0.x, p0.y}; ss[1] = f32x2{p0.z, p0.w}; ss[2] = f32x2{p1.x, p1.y}; ss[3] = f32x2{p1.z, p1.w};
        }
#endif
        f32x2 gx[4];
#pragma unroll
        for (int j = 0; j < 4; ++j) gx[j] = f32x2{__builtin_amdgcn_sqrtf(ss[j].x), __builtin_amdgcn_sqrtf(ss[j].y)};
        f32x2 t2 = (gx[0] + gx[1]) + (gx[2] + gx[3]);
        const float tot = wave_sum(t2.x + t2.y);
        const float inv = __builtin_amdgcn_rcpf(__builtin_fmaf(tot, 1.0f / D, 1e-6f));
        f32x2 sc[4], bb[4];
#pragma unroll
        for (int j = 0; j < 4; ++j) {
            sc[j] = fma2(unpack2(gv[j]) * gx[j], f32x2{inv, inv}, f32x2{1.f, 1.f});
            bb[j] = unpack2(bv[j]);
        }
#pragma unroll
        for (int r = 0; r < RPW; ++r) {
            u32x4 o;
#pragma unroll
            for (int j = 0; j < 4; ++j) o[j] = pack2(fma2(f[r][j], sc[j], bb[j]));
            __builtin_amdgcn_raw_buffer_store_b128(o, ro, base + it * STEP + r * 1024, 0, ST_AUX);
        }
    }
#if PROBE
#if PROBE == 5 || PROBE == 8
    tx = rt();
#endif
    asm volatile("s_waitcnt vmcnt(0)" ::: "memory");
#if PROBE == 3
    tx = rt();
#endif
    const u64 lim = t0 + 3000 + ((PROBE == 6 || PROBE == 8) ? 8 : 2) * (tx - ty);
    for (int i = 0; i < 100000; ++i) { if (rt() >= lim) break; __builtin_amdgcn_s_sleep(2); }
#endif
}

extern "C" void kernel_launch(void* const* d_in, const int* in_sizes, int n_in,
                              void* d_out, int out_size, void* d_ws, size_t ws_size,
                              hipStream_t stream) {
    grn_kernel<<<1024 / NSEQ, THREADS, 0, stream>>>((const char*)d_in[0], (const u32x4*)d_in[1], (const u32x4*)d_in[2], (char*)d_out);
}
```

```cpp
#include <hip/hip_runtime.h>

#define T1 @T1@
#define N1 @N1@
#define T2 @T2@
#define TIMELINE @TL@

typedef unsigned int u32;
typedef int i32;
typedef unsigned long long u64;
typedef u32 u32x4 __attribute__((ext_vector_type(4)));
typedef float f32x2 __attribute__((ext_vector_type(2)));
typedef float f32x4 __attribute__((ext_vector_type(4)));
typedef __bf16 bf16x2 __attribute__((ext_vector_type(2)));
typedef __attribute__((address_space(1))) const u32x4* gptr4;

constexpr int S = 32, D = 512;
constexpr int WPB = 4, RPW = S / WPB, THREADS = 64 * WPB;

__device__ __forceinline__ f32x2 unpack2(u32 v) {
    return f32x2{__builtin_bit_cast(float, v << 16), __builtin_bit_cast(float, v & 0xffff0000u)};
}
__device__ __forceinline__ u32 pack2(f32x2 f) { return __builtin_bit_cast(u32, __builtin_convertvector(f, bf16x2)); }
__device__ __forceinline__ f32x2 fma2(f32x2 a, f32x2 b, f32x2 c) { return __builtin_elementwise_fma(a, b, c); }
template <int CTRL>
__device__ __forceinline__ float dpp_add(float v) {
    return v + __builtin_bit_cast(float, __builtin_amdgcn_update_dpp(0, __builtin_bit_cast(int, v), CTRL, 0xf, 0xf, true));
}
__device__ __forceinline__ float wave_sum(float v) {
    v = dpp_add<0xB1>(v);
    v = dpp_add<0x4E>(v);
    v = dpp_add<0x141>(v);
    v = dpp_add<0x140>(v);
    const int iv = __builtin_bit_cast(int, v);
    float a = __builtin_bit_cast(float, __builtin_amdgcn_readlane(iv, 0));
    float b = __builtin_bit_cast(float, __builtin_amdgcn_readlane(iv, 16));
    float c = __builtin_bit_cast(float, __builtin_amdgcn_readlane(iv, 32));
    float d = __builtin_bit_cast(float, __builtin_amdgcn_readlane(iv, 48));
    return (a + b) + (c + d);
}
__device__ __forceinline__ u32 now() { return (u32)__builtin_amdgcn_s_memrealtime(); }

#define STR2(x) #x
#define STR(x) STR2(x)
#define WAIT_UNTIL(lbl, T)                      \
    lbl ":\n\t"                                 \
    "s_sleep 1\n\t"                             \
    "s_memrealtime vcc\n\t"                     \
    "s_waitcnt lgkmcnt(0)\n\t"                  \
    "s_sub_i32 vcc_lo, vcc_lo, %[t0]\n\t"       \
    "s_cmpk_lt_i32 vcc_lo, " STR(T) "\n\t"      \
    "s_cbranch_scc1 " lbl "b\n\t"
#define ST(k, off) "global_store_dwordx4 %[vo], %[d" #k "], %[ob] offset:" #off " sc1\n\t"

__global__ __launch_bounds__(THREADS) void grn_kernel(const char* __restrict__ x, const u32x4* __restrict__ g,
                                                      const u32x4* __restrict__ bia, char* __restrict__ out
#if TIMELINE
                                                      , u32* ws
#endif
                                                      ) {
    const u32 lane16 = (threadIdx.x & 63) * 16;
    const u32 wv = threadIdx.x >> 6;
    const u32 mid = (blockIdx.x * (S * D * 2) + 4096) | (wv << 13) | lane16;
    __shared__ f32x4 red[WPB][2][64];
    asm volatile("" :: "s"(x), "v"(mid));
    __builtin_amdgcn_sched_barrier(0);
    u64 t064 = __builtin_amdgcn_s_memrealtime();
    u32x4 v[RPW];
#pragma unroll
    for (int r = 0; r < RPW; ++r) v[r] = *(gptr4)(x + mid + (r - 4) * 1024);
    const u32x4 gv = *(gptr4)((const char*)g + lane16), bv = *(gptr4)((const char*)bia + lane16);
    __builtin_amdgcn_sched_barrier(0);

    f32x2 ss[4], f[RPW][4];
#pragma unroll
    for (int r = 0; r < RPW; ++r)
#pragma unroll
        for (int j = 0; j < 4; ++j) {
            f[r][j] = unpack2(v[r][j]);
            ss[j] = r == 0 ? f[r][j] * f[r][j] : fma2(f[r][j], f[r][j], ss[j]);
        }
    const u32 lane = threadIdx.x & 63;
    red[wv][0][lane] = f32x4{ss[0].x, ss[0].y, ss[1].x, ss[1].y};
    red[wv][1][lane] = f32x4{ss[2].x, ss[2].y, ss[3].x, ss[3].y};
    asm volatile("s_waitcnt lgkmcnt(0)" ::: "memory");
    __builtin_amdgcn_s_barrier();
    asm volatile("" ::: "memory");
#if TIMELINE
    __builtin_amdgcn_sched_barrier(0);
    const u32 tld = now();
    asm volatile("s_waitcnt lgkmcnt(0)" ::: "memory");
    __builtin_amdgcn_sched_barrier(0);
#endif
    {
        f32x4 p0 = red[0][0][lane], p1 = red[0][1][lane];
#pragma unroll
        for (int w = 1; w < WPB; ++w) { p0 += red[w][0][lane]; p1 += red[w][1][lane]; }
        ss[0] = f32x2{p0.x, p0.y}; ss[1] = f32x2{p0.z, p0.w}; ss[2] = f32x2{p1.x, p1.y}; ss[3] = f32x2{p1.z, p1.w};
    }
    f32x2 gx[4];
#pragma unroll
    for (int j = 0; j < 4; ++j) gx[j] = f32x2{__builtin_amdgcn_sqrtf(ss[j].x), __builtin_amdgcn_sqrtf(ss[j].y)};
    f32x2 t2 = (gx[0] + gx[1]) + (gx[2] + gx[3]);
    const float tot = wave_sum(t2.x + t2.y);
    const float inv = __builtin_amdgcn_rcpf(__builtin_fmaf(tot, 1.0f / D, 1e-6f));
    f32x2 sc[4], bb[4];
#pragma unroll
    for (int j = 0; j < 4; ++j) {
        sc[j] = fma2(unpack2(gv[j]) * gx[j], f32x2{inv, inv}, f32x2{1.f, 1.f});
        bb[j] = unpack2(bv[j]);
    }
#pragma unroll
    for (int r = 0; r < RPW; ++r) {
        u32x4 o;
#pragma unroll
        for (int j = 0; j < 4; ++j) o[j] = pack2(fma2(f[r][j], sc[j], bb[j]));
        v[r] = o;
    }
    asm volatile("" : "+s"(t064));
    const u32 t0 = (u32)t064;
#if TIMELINE
    const u32 tst = now();
#endif
    asm volatile(
        "s_nop 1\n\t"
        ".p2align 6\n\t"
#if N1 > 0
        WAIT_UNTIL("1", T1)
        ST(0, -4096)
#if N1 > 1
        ST(1, -3072)
#endif
#if N1 > 2
        ST(2, -2048)
#endif
        ".p2align 6\n\t"
#endif
        WAIT_UNTIL("2", T2)
#if N1 < 1
        ST(0, -4096)
#endif
#if N1 < 2
        ST(1, -3072)
#endif
#if N1 < 3
        ST(2, -2048)
#endif
        ST(3, -1024) ST(4, 0) ST(5, 1024) ST(6, 2048) ST(7, 3072)
        "s_nop 1"
        :
        : [t0] "s"(t0), [vo] "v"(mid), [ob] "s"(out), [d0] "v"(v[0]), [d1] "v"(v[1]), [d2] "v"(v[2]), [d3] "v"(v[3]),
          [d4] "v"(v[4]), [d5] "v"(v[5]), [d6] "v"(v[6]), [d7] "v"(v[7])
        : "memory", "vcc", "scc");

    if (blockIdx.x == 0 && threadIdx.x == 0) {
        while ((i32)(now() - t0) < 1500) __builtin_amdgcn_s_sleep(4);
        u32 code = 0;
        for (int which = 0; which < 4; ++which) {
            const char* bp = ((which & 1) ? (const char*)out : x) + 32768 + (which >> 1) * (20u << 20);
            u32 sum[2] = {0, 0};
            for (int k = 0; k < 10; ++k)
                for (int h = 0; h < 2; ++h) {
                    const char* p = bp + h * 8192 + k * 768;
                    u64 a, b; u32 w;
                    asm volatile("s_memtime %0\n\ts_waitcnt lgkmcnt(0)\n\tglobal_load_dword %2, %3, off\n\ts_waitcnt vmcnt(0)\n\ts_memtime %1\n\ts_waitcnt lgkmcnt(0)"
                                 : "=&s"(a), "=&s"(b), "=&v"(w) : "v"(p) : "memory");
                    if (k >= 2) sum[h] += (u32)(b - a);
                }
            code |= (sum[0] > sum[1] ? 1u : 0u) << which;
        }
        const i32 lim = 6000 + ((code ^ (code >> 1)) & 1) * 1000 + ((code ^ (code >> 2)) & 1) * 2000 + (((code >> 1) ^ (code >> 3)) & 1) * 4000 + (code & 1) * 500;
        while ((i32)(now() - t0) < lim) __builtin_amdgcn_s_sleep(4);
    }
#if TIMELINE
    asm volatile("s_waitcnt vmcnt(0)" ::: "memory");
    const u32 tend = now();
    u32 xcc; asm volatile("s_getreg_b32 %0, hwreg(HW_REG_XCC_ID)" : "=s"(xcc));
    if (threadIdx.x == 0) { u32* p = ws + 1024 + blockIdx.x * 8; p[0] = t0; p[1] = tld; p[2] = tst; p[3] = tend; p[4] = xcc & 15; p[5] = 0x7117e11e; }
#endif
}

extern "C" void kernel_launch(void* const* d_in, const int* in_sizes, int n_in,
                              void* d_out, int out_size, void* d_ws, size_t ws_size,
                              hipStream_t stream) {
    grn_kernel<<<1024, THREADS, 0, stream>>>((const char*)d_in[0], (const u32x4*)d_in[1], (const u32x4*)d_in[2], (char*)d_out
#if TIMELINE
                                             , (u32*)d_ws
#endif
                                             );
}
```

```cpp
#include <hip/hip_runtime.h>

typedef unsigned int u32;
typedef unsigned long long u64;
typedef u32 u32x4 __attribute__((ext_vector_type(4)));
typedef float f32x2 __attribute__((ext_vector_type(2)));
typedef float f32x4 __attribute__((ext_vector_type(4)));
typedef __bf16 bf16x2 __attribute__((ext_vector_type(2)));
typedef __attribute__((address_space(1))) const u32x4* gptr4;

constexpr int S = 32, D = 512;
constexpr int WAVES = 4, RPW = S / WAVES, THREADS = 64 * WAVES;

constexpr u32 LD_STEP16 = 412, ST_START = 448, ST_STEP16 = 412;

__device__ __forceinline__ f32x2 unpack2(u32 v) {
    return f32x2{__builtin_bit_cast(float, v << 16), __builtin_bit_cast(float, v & 0xffff0000u)};
}
__device__ __forceinline__ u32 pack2(f32x2 f) { return __builtin_bit_cast(u32, __builtin_convertvector(f, bf16x2)); }
__device__ __forceinline__ f32x2 fma2(f32x2 a, f32x2 b, f32x2 c) { return __builtin_elementwise_fma(a, b, c); }
template <int CTRL>
__device__ __forceinline__ float dpp_add(float v) {
    return v + __builtin_bit_cast(float, __builtin_amdgcn_update_dpp(0, __builtin_bit_cast(int, v), CTRL, 0xf, 0xf, true));
}
__device__ __forceinline__ float wave_sum(float v) {
    v = dpp_add<0xB1>(v);
    v = dpp_add<0x4E>(v);
    v = dpp_add<0x141>(v);
    v = dpp_add<0x140>(v);
    const int iv = __builtin_bit_cast(int, v);
    const float a = __builtin_bit_cast(float, __builtin_amdgcn_readlane(iv, 0));
    const float b = __builtin_bit_cast(float, __builtin_amdgcn_readlane(iv, 16));
    const float c = __builtin_bit_cast(float, __builtin_amdgcn_readlane(iv, 32));
    const float d = __builtin_bit_cast(float, __builtin_amdgcn_readlane(iv, 48));
    return (a + b) + (c + d);
}

#define ST(k, off) "global_store_dwordx4 %[vo], %[d" #k "], %[ob] offset:" #off " sc1\n\t"

__global__ __launch_bounds__(THREADS) __attribute__((aligned(4096)))
void grn_kernel(const char* __restrict__ x, const char* __restrict__ g, const char* __restrict__ bia,
                char* __restrict__ out) {
    __shared__ f32x4 red[WAVES][2][64];
    const u32 lane = threadIdx.x & 63, lane16 = lane * 16;
    const u32 wv = threadIdx.x >> 6;
    const u32 vo = (blockIdx.x * (S * D * 2) + 4096) | (wv << 13) | lane16;
    const u32 q = ((blockIdx.x >> 8) & 3) * 4 + __builtin_amdgcn_readfirstlane(wv);
    const u32 tq = q * LD_STEP16 >> 4;
    asm volatile("" :: "v"(vo), "v"(lane16), "s"(tq));
    __builtin_amdgcn_sched_barrier(0);

    u64 t0;
    u32 tlo;
    asm volatile(
        "s_memrealtime %[t0]\n\t"
        "s_cmp_eq_u32 %[tq], 0\n\t"
        "s_cbranch_scc1 2f\n\t"
        "s_waitcnt lgkmcnt(0)\n\t"
        "s_mov_b64 vcc, %[t0]\n\t"
        "s_mov_b32 %[lo], vcc_lo\n"
        "1:\n\t"
        "s_sleep 1\n\t"
        "s_memrealtime vcc\n\t"
        "s_waitcnt lgkmcnt(0)\n\t"
        "s_sub_u32 vcc_lo, vcc_lo, %[lo]\n\t"
        "s_cmp_lt_u32 vcc_lo, %[tq]\n\t"
        "s_cbranch_scc1 1b\n"
        "2:"
        : [t0] "=&s"(t0), [lo] "=&s"(tlo) : [tq] "s"(tq), "s"(x) : "vcc", "scc");
    __builtin_amdgcn_sched_barrier(0);

    u32x4 v[RPW];
#pragma unroll
    for (int r = 0; r < RPW; ++r) v[r] = __builtin_nontemporal_load((gptr4)(x + vo + (r - 4) * 1024LL));
    const u32x4 gv = *(gptr4)(g + lane16), bv = *(gptr4)(bia + lane16);
    __builtin_amdgcn_sched_barrier(0);

    f32x2 ss[4], f[RPW][4];
#pragma unroll
    for (int r = 0; r < RPW; ++r)
#pragma unroll
        for (int j = 0; j < 4; ++j) {
            f[r][j] = unpack2(v[r][j]);
            ss[j] = r == 0 ? f[r][j] * f[r][j] : fma2(f[r][j], f[r][j], ss[j]);
        }

    red[wv][0][lane] = f32x4{ss[0].x, ss[0].y, ss[1].x, ss[1].y};
    red[wv][1][lane] = f32x4{ss[2].x, ss[2].y, ss[3].x, ss[3].y};
    asm volatile("s_waitcnt lgkmcnt(0)" : "+s"(t0) :: "memory");
    __builtin_amdgcn_s_barrier();
    asm volatile("" ::: "memory");
    f32x4 p0 = red[0][0][lane], p1 = red[0][1][lane];
#pragma unroll
    for (int w = 1; w < WAVES; ++w) { p0 += red[w][0][lane]; p1 += red[w][1][lane]; }

    const f32x2 gx[4] = {f32x2{__builtin_amdgcn_sqrtf(p0.x), __builtin_amdgcn_sqrtf(p0.y)},
                         f32x2{__builtin_amdgcn_sqrtf(p0.z), __builtin_amdgcn_sqrtf(p0.w)},
                         f32x2{__builtin_amdgcn_sqrtf(p1.x), __builtin_amdgcn_sqrtf(p1.y)},
                         f32x2{__builtin_amdgcn_sqrtf(p1.z), __builtin_amdgcn_sqrtf(p1.w)}};
    const f32x2 t2 = (gx[0] + gx[1]) + (gx[2] + gx[3]);
    const float tot = wave_sum(t2.x + t2.y);
    const float inv = 1.0f / __builtin_fmaf(tot, 1.0f / D, 1e-6f);
    f32x2 sc[4], bb[4];
#pragma unroll
    for (int j = 0; j < 4; ++j) {
        sc[j] = fma2(unpack2(gv[j]), gx[j] * f32x2{inv, inv}, f32x2{1.f, 1.f});
        bb[j] = unpack2(bv[j]);
    }
#pragma unroll
    for (int r = 0; r < RPW; ++r) {
        u32x4 o;
#pragma unroll
        for (int j = 0; j < 4; ++j) o[j] = pack2(fma2(f[r][j], sc[j], bb[j]));
        v[r] = o;
    }

    const u32 tw = ST_START + (q * ST_STEP16 >> 4);
    asm volatile(
        ".p2align 7\n"
        "1:\n\t"
        "s_sleep 1\n\t"
        "s_memrealtime vcc\n\t"
        "s_waitcnt lgkmcnt(0)\n\t"
        "s_sub_u32 vcc_lo, vcc_lo, %[t0]\n\t"
        "s_cmp_lt_u32 vcc_lo, %[tw]\n\t"
        "s_cbranch_scc1 1b\n\t"
        ST(0, -4096) ST(1, -3072) ST(2, -2048) ST(3, -1024) ST(4, 0) ST(5, 1024) ST(6, 2048) ST(7, 3072)
        "s_nop 1"
        :
        : [t0] "s"((u32)t0), [tw] "s"(tw), [ob] "s"(out), [vo] "v"(vo),
          [d0] "v"(v[0]), [d1] "v"(v[1]), [d2] "v"(v[2]), [d3] "v"(v[3]),
          [d4] "v"(v[4]), [d5] "v"(v[5]), [d6] "v"(v[6]), [d7] "v"(v[7])
        : "memory", "vcc", "scc");
}

extern "C" void kernel_launch(void* const* d_in, const int* in_sizes, int n_in,
                              void* d_out, int out_size, void* d_ws, size_t ws_size,
                              hipStream_t stream) {
    const int batches = in_sizes[0] / (S * D);
    grn_kernel<<<batches, THREADS, 0, stream>>>((const char*)d_in[0], (const char*)d_in[1], (const char*)d_in[2],
                                                (char*)d_out);
}
```
